```python
import torch

def _softmax(x, axis=-1):
    return torch.softmax(x, dim=axis)
def _gelu(x):
    return torch.nn.functional.gelu(x, approximate="tanh")
def _silu(x):
    return torch.nn.functional.silu(x)
def _sigmoid(x):
    return torch.sigmoid(x)
def _layernorm(x, axis=-1, eps=1e-5):
    m = x.mean(dim=axis, keepdim=True)
    v = x.var(dim=axis, keepdim=True, unbiased=False)
    return (x - m) / torch.sqrt(v + eps)
def _scatter_add(tgt, idx, src, axis=0):
    return tgt.clone().index_add_(axis, idx, src)
def _segment_sum(src, seg, n):
    out = torch.zeros((n,) + src.shape[1:], dtype=src.dtype)
    return out.index_add_(0, seg, src)
def _pad(x, pw):
    flat = tuple(v for pair in reversed(pw) for v in pair)
    return torch.nn.functional.pad(x, flat)

kernel_name = 'standalone__rezero__sampling'

def setup_inputs():

    _w = (lambda a: (lambda t: t.to(torch.bfloat16) if t.is_floating_point() else t)(torch.as_tensor(a)))
    B, S, D = 1024, 8, 640
    return {
        "x": _w((lambda *s: torch.randn(*s, dtype=torch.bfloat16) * 0.05)(B, S, D)),
        "fx": _w((lambda *s: torch.randn(*s, dtype=torch.bfloat16) * 0.8)(B, S, D)),
        "alpha": _w((lambda *s: torch.randn(*s, dtype=torch.bfloat16) * 0.8)(1) * 0.1),
    }

def reference(x, fx, alpha):
    return x + alpha * fx
```

```cpp
#include <hip/hip_runtime.h>
#include <hip/hip_bf16.h>
#include <stdint.h>

typedef uint32_t u32x4 __attribute__((ext_vector_type(4)));
typedef float f32x2 __attribute__((ext_vector_type(2)));
typedef __bf16 bf16x2 __attribute__((ext_vector_type(2)));

#define BLOCK 256
#define VPT 2

__device__ __forceinline__ uint32_t rezero2(uint32_t x2, uint32_t f2, float a) {
    float f_lo = __uint_as_float(f2 << 16);
    float f_hi = __uint_as_float(f2 & 0xffff0000u);
    f32x2 p = {a * f_lo, a * f_hi};
    bf16x2 pb = __builtin_convertvector(p, bf16x2);
    uint32_t t2 = __builtin_bit_cast(uint32_t, pb);
    float t_lo = __uint_as_float(t2 << 16);
    float t_hi = __uint_as_float(t2 & 0xffff0000u);
    float x_lo = __uint_as_float(x2 << 16);
    float x_hi = __uint_as_float(x2 & 0xffff0000u);
    f32x2 s = {x_lo + t_lo, x_hi + t_hi};
    bf16x2 sb = __builtin_convertvector(s, bf16x2);
    return __builtin_bit_cast(uint32_t, sb);
}

__device__ __forceinline__ u32x4 rezero8(u32x4 x, u32x4 f, float a) {
    u32x4 o;
    o.x = rezero2(x.x, f.x, a);
    o.y = rezero2(x.y, f.y, a);
    o.z = rezero2(x.z, f.z, a);
    o.w = rezero2(x.w, f.w, a);
    return o;
}

__global__ __launch_bounds__(BLOCK) void rezero_kernel(const u32x4* __restrict__ x,
                                                       const u32x4* __restrict__ fx,
                                                       const uint16_t* __restrict__ alpha,
                                                       u32x4* __restrict__ out, int nvec, int n) {
    const float a = __uint_as_float(((uint32_t)alpha[0]) << 16);
    const int base = blockIdx.x * (BLOCK * VPT) + threadIdx.x;
    if (base + (VPT - 1) * BLOCK < nvec) {
        u32x4 xv[VPT], fv[VPT];
#pragma unroll
        for (int i = 0; i < VPT; ++i) {
            xv[i] = x[base + i * BLOCK];
            fv[i] = fx[base + i * BLOCK];
        }
#pragma unroll
        for (int i = 0; i < VPT; ++i) out[base + i * BLOCK] = rezero8(xv[i], fv[i], a);
    } else {
#pragma unroll
        for (int i = 0; i < VPT; ++i) {
            int idx = base + i * BLOCK;
            if (idx < nvec) out[idx] = rezero8(x[idx], fx[idx], a);
        }
    }
    if (blockIdx.x == 0 && threadIdx.x < (n & 7)) {
        int e = (nvec << 3) + threadIdx.x;
        const uint16_t* xs = (const uint16_t*)x;
        const uint16_t* fs = (const uint16_t*)fx;
        uint16_t* os = (uint16_t*)out;
        os[e] = (uint16_t)(rezero2(xs[e], fs[e], a) & 0xffffu);
    }
}

extern "C" void kernel_launch(void* const* d_in, const int* in_sizes, int n_in, void* d_out,
                              int out_size, void* d_ws, size_t ws_size, hipStream_t stream) {
    (void)n_in; (void)d_ws; (void)ws_size; (void)in_sizes;
    const int n = out_size;
    const int nvec = n >> 3;
    int grid = (nvec + BLOCK * VPT - 1) / (BLOCK * VPT);
    if (grid < 1) grid = 1;
    rezero_kernel<<<grid, BLOCK, 0, stream>>>((const u32x4*)d_in[0], (const u32x4*)d_in[1],
                                              (const uint16_t*)d_in[2], (u32x4*)d_out, nvec, n);
}
```

```cpp
#include <hip/hip_runtime.h>
#include <hip/hip_bf16.h>
#include <stdint.h>
#include <stdio.h>

typedef uint32_t u32x4 __attribute__((ext_vector_type(4)));
typedef float f32x2 __attribute__((ext_vector_type(2)));
typedef __bf16 bf16x2 __attribute__((ext_vector_type(2)));
#define BLOCK 256
#define VPT 2
__device__ __forceinline__ uint32_t rezero2(uint32_t x2, uint32_t f2, float a) {
    float f_lo = __uint_as_float(f2 << 16);
    float f_hi = __uint_as_float(f2 & 0xffff0000u);
    f32x2 p = {a * f_lo, a * f_hi};
    bf16x2 pb = __builtin_convertvector(p, bf16x2);
    uint32_t t2 = __builtin_bit_cast(uint32_t, pb);
    float t_lo = __uint_as_float(t2 << 16);
    float t_hi = __uint_as_float(t2 & 0xffff0000u);
    float x_lo = __uint_as_float(x2 << 16);
    float x_hi = __uint_as_float(x2 & 0xffff0000u);
    f32x2 s = {x_lo + t_lo, x_hi + t_hi};
    bf16x2 sb = __builtin_convertvector(s, bf16x2);
    return __builtin_bit_cast(uint32_t, sb);
}
__device__ __forceinline__ u32x4 rezero8(u32x4 x, u32x4 f, float a) {
    u32x4 o;
    o.x = rezero2(x.x, f.x, a); o.y = rezero2(x.y, f.y, a);
    o.z = rezero2(x.z, f.z, a); o.w = rezero2(x.w, f.w, a);
    return o;
}
__global__ __launch_bounds__(BLOCK) void rezero_fast(const u32x4* __restrict__ x, const u32x4* __restrict__ fx,
                                                 const uint16_t* __restrict__ alpha, u32x4* __restrict__ out) {
    const uint32_t base = blockIdx.x * (BLOCK * VPT) + threadIdx.x;
    u32x4 xv[VPT], fv[VPT];
#pragma unroll
    for (int i = 0; i < VPT; ++i) { fv[i] = fx[base + i * BLOCK]; xv[i] = x[base + i * BLOCK]; }
    float a = __uint_as_float((uint32_t)alpha[0] << 16);
#pragma unroll
    for (int i = 0; i < VPT; ++i) out[base + i * BLOCK] = rezero8(xv[i], fv[i], a);
}
static void pattr(const char* nm, void* p) {
    hipPointerAttribute_t at; hipError_t e = hipPointerGetAttributes(&at, p);
    void* base = 0; size_t sz = 0; hipError_t e2 = hipMemGetAddressRange((hipDeviceptr_t*)&base, &sz, p);
    fprintf(stderr, "DIAG %s ptr=%p err=%d type=%d dev=%d managed=%d allocFlags=0x%x | range err=%d base=%p size=%zu\n", nm, p, (int)e,
            (int)at.type, at.device, at.isManaged, at.allocationFlags, (int)e2, base, sz);
}
extern "C" void kernel_launch(void* const* d_in, const int* in_sizes, int n_in, void* d_out,
                              int out_size, void* d_ws, size_t ws_size, hipStream_t stream) {
    pattr("x", d_in[0]); pattr("fx", d_in[1]); pattr("alpha", d_in[2]); pattr("out", d_out); pattr("ws", d_ws);
    fprintf(stderr, "DIAG stream=%p\n", (void*)stream);
    const size_t n = (size_t)out_size;
    rezero_fast<<<(unsigned)(n / (BLOCK * VPT * 8)), BLOCK, 0, stream>>>((const u32x4*)d_in[0], (const u32x4*)d_in[1], (const uint16_t*)d_in[2], (u32x4*)d_out);
}
```

```cpp
#include <hip/hip_runtime.h>
#include <stdint.h>

typedef uint32_t u32x4 __attribute__((ext_vector_type(4)));
typedef float f32x2 __attribute__((ext_vector_type(2)));
typedef __bf16 bf16x2 __attribute__((ext_vector_type(2)));

#define RZ_BLOCK 256
#define RZ_VPT 2

__device__ __forceinline__ uint32_t rezero2(uint32_t x2, uint32_t f2, float a) {
    const float f_lo = __uint_as_float(f2 << 16);
    const float f_hi = __uint_as_float(f2 & 0xffff0000u);
    const f32x2 p = {a * f_lo, a * f_hi};
    const uint32_t t2 = __builtin_bit_cast(uint32_t, __builtin_convertvector(p, bf16x2));
    const float t_lo = __uint_as_float(t2 << 16);
    const float t_hi = __uint_as_float(t2 & 0xffff0000u);
    const float x_lo = __uint_as_float(x2 << 16);
    const float x_hi = __uint_as_float(x2 & 0xffff0000u);
    const f32x2 s = {x_lo + t_lo, x_hi + t_hi};
    return __builtin_bit_cast(uint32_t, __builtin_convertvector(s, bf16x2));
}

__device__ __forceinline__ u32x4 rezero8(u32x4 x, u32x4 f, float a) {
    u32x4 o;
    o.x = rezero2(x.x, f.x, a);
    o.y = rezero2(x.y, f.y, a);
    o.z = rezero2(x.z, f.z, a);
    o.w = rezero2(x.w, f.w, a);
    return o;
}

__global__ __launch_bounds__(RZ_BLOCK) void rezero_fast(const char* __restrict__ x,
                                                        const char* __restrict__ fx,
                                                        char* __restrict__ out,
                                                        const uint32_t* __restrict__ alpha) {
    const uint32_t off = (blockIdx.x * (RZ_BLOCK * RZ_VPT) + threadIdx.x) * 16u;
    u32x4 xv[RZ_VPT], fv[RZ_VPT];
#pragma unroll
    for (int i = 0; i < RZ_VPT; ++i) {
        fv[i] = *(const u32x4*)(fx + (off + i * (RZ_BLOCK * 16u)));
        xv[i] = *(const u32x4*)(x + (off + i * (RZ_BLOCK * 16u)));
    }
    const uint32_t aw = *(const __attribute__((address_space(4))) uint32_t*)alpha;
    const float a = __uint_as_float(aw << 16);
    const __amdgpu_buffer_rsrc_t ro =
        __builtin_amdgcn_make_buffer_rsrc(out, 0, 0x80000000u, 0x00020000);
#pragma unroll
    for (int i = 0; i < RZ_VPT; ++i)
        __builtin_amdgcn_raw_buffer_store_b128(rezero8(xv[i], fv[i], a), ro,
                                               off + i * (RZ_BLOCK * 16u), 0,   17);
}

__global__ __launch_bounds__(256) void rezero_generic(const uint16_t* __restrict__ x,
                                                      const uint16_t* __restrict__ fx,
                                                      uint16_t* __restrict__ out,
                                                      const uint16_t* __restrict__ alpha, size_t n,
                                                      size_t stride) {
    const float a = __uint_as_float(((uint32_t)alpha[0]) << 16);
    for (size_t i = (size_t)blockIdx.x * 256 + threadIdx.x; i < n; i += stride)
        out[i] = (uint16_t)(rezero2(x[i], fx[i], a) & 0xffffu);
}

extern "C" void kernel_launch(void* const* d_in, const int* in_sizes, int n_in, void* d_out,
                              int out_size, void* d_ws, size_t ws_size, hipStream_t stream) {
    (void)in_sizes; (void)n_in; (void)d_ws; (void)ws_size;
    if (out_size <= 0) return;
    const size_t n = (size_t)out_size;
    const size_t per_block = (size_t)RZ_BLOCK * RZ_VPT * 8;
    const uintptr_t al = (uintptr_t)d_in[0] | (uintptr_t)d_in[1] | (uintptr_t)d_out;
    if (n % per_block == 0 && (al & 15) == 0 && ((uintptr_t)d_in[2] & 3) == 0 &&
        n * 2 <= (size_t)0x80000000u) {
        rezero_fast<<<(unsigned)(n / per_block), RZ_BLOCK, 0, stream>>>(
            (const char*)d_in[0], (const char*)d_in[1], (char*)d_out, (const uint32_t*)d_in[2]);
    } else {
        size_t g = (n + 255) / 256;
        if (g > 4096) g = 4096;
        rezero_generic<<<(unsigned)g, 256, 0, stream>>>((const uint16_t*)d_in[0],
                                                        (const uint16_t*)d_in[1], (uint16_t*)d_out,
                                                        (const uint16_t*)d_in[2], n, g * 256);
    }
}
```
